```python
import torch

def _softmax(x, axis=-1):
    return torch.softmax(x, dim=axis)
def _gelu(x):
    return torch.nn.functional.gelu(x, approximate="tanh")
def _silu(x):
    return torch.nn.functional.silu(x)
def _sigmoid(x):
    return torch.sigmoid(x)
def _layernorm(x, axis=-1, eps=1e-5):
    m = x.mean(dim=axis, keepdim=True)
    v = x.var(dim=axis, keepdim=True, unbiased=False)
    return (x - m) / torch.sqrt(v + eps)
def _scatter_add(tgt, idx, src, axis=0):
    return tgt.clone().index_add_(axis, idx, src)
def _segment_sum(src, seg, n):
    out = torch.zeros((n,) + src.shape[1:], dtype=src.dtype)
    return out.index_add_(0, seg, src)
def _pad(x, pw):
    flat = tuple(v for pair in reversed(pw) for v in pair)
    return torch.nn.functional.pad(x, flat)

kernel_name = 'sequential_preLN__sliding_block_cached__dense_activation_gelu__decode'

def setup_inputs():

    _w = (lambda a: (lambda t: t.to(torch.bfloat16) if t.is_floating_point() else t)(torch.as_tensor(a)))
    def _A_setup():
        B, Sq, D = 8, 1, 512
        Hq, Hkv, Sk, Dh = 32, 32, 4096, 128
        assert Hq % Hkv == 0, (Hq, Hkv)
        return {
            "x": _w((lambda *s: torch.randn(*s, dtype=torch.bfloat16) * 0.05)(B, Sq, D)),
            "k_cache": _w((lambda *s: torch.randn(*s, dtype=torch.bfloat16) * 1.6)(B, Hkv, Sk, Dh)),
            "v_cache": _w((lambda *s: torch.randn(*s, dtype=torch.bfloat16) * 0.4)(B, Hkv, Sk, Dh)),
            "wq": _w((lambda *s: torch.randn(*s, dtype=torch.bfloat16) * 1.6)(D, Hq * Dh) * (D ** -0.5)),
            "wo": _w((lambda *s: torch.randn(*s, dtype=torch.bfloat16) * 0.4)(Hq * Dh, D) * ((Hq * Dh) ** -0.5)),
        }
    _A_in = _A_setup()

    def _B_setup():
        B, S, D, F = 8, 1, 512, 1376
        return {
            "x": _w((lambda *s: torch.randn(*s, dtype=torch.bfloat16) * 0.05)(B, S, D)),
            "w1": _w((lambda *s: torch.randn(*s, dtype=torch.bfloat16) * 0.8)(D, F) * (D ** -0.5)),
            "b1": _w((lambda *s: torch.randn(*s, dtype=torch.bfloat16) * 0.8)(F) * 0.01),
            "w2": _w((lambda *s: torch.randn(*s, dtype=torch.bfloat16) * 0.1)(F, D) * (F ** -0.5)),
            "b2": _w((lambda *s: torch.randn(*s, dtype=torch.bfloat16) * 0.1)(D) * 0.01),
        }
    _B_in = _B_setup()

    return {
        "A_x": _A_in["x"],
        "A_k_cache": _A_in["k_cache"],
        "A_v_cache": _A_in["v_cache"],
        "A_wq": _A_in["wq"],
        "A_wo": _A_in["wo"],
        "B_x": _B_in["x"],
        "B_w1": _B_in["w1"],
        "B_b1": _B_in["b1"],
        "B_w2": _B_in["w2"],
        "B_b2": _B_in["b2"],
    }

def reference(A_x, A_k_cache, A_v_cache, A_wq, A_wo, B_x, B_w1, B_b1, B_w2, B_b2):
    def _A_ref(x, k_cache, v_cache, wq, wo):
        B, Sq, D = x.shape
        Hq, Dh = 32, 128
        Hkv, Sk = k_cache.shape[1], k_cache.shape[2]
        W = min(4096, max(256, Sk // 4))
        rep = Hq // Hkv
        q = (lambda x, *d: x.permute(*d))((x @ wq).reshape(B, Sq, Hq, Dh), 0, 2, 1, 3)
        k = (lambda x, n, axis: torch.repeat_interleave(x, n, dim=axis))(k_cache[:, :, Sk - W:, :], rep, axis=1)
        v = (lambda x, n, axis: torch.repeat_interleave(x, n, dim=axis))(v_cache[:, :, Sk - W:, :], rep, axis=1)
        scores = torch.einsum("bhqd,bhkd->bhqk", q, k) * (Dh ** -0.5)
        p = _softmax(scores, axis=-1)
        o = torch.einsum("bhqk,bhkd->bhqd", p, v)
        return (lambda x, *d: x.permute(*d))(o, 0, 2, 1, 3).reshape(B, Sq, Hq * Dh) @ wo

    def _B_ref(x, w1, b1, w2, b2):
        h = torch.einsum("bsd,df->bsf", x, w1) + b1
        h = _gelu(h)
        return torch.einsum("bsf,fd->bsd", h, w2) + b2

    x = A_x
    h = x + _A_ref(_layernorm(x, axis=-1), A_k_cache, A_v_cache, A_wq, A_wo)
    h2 = h + _B_ref(_layernorm(h, axis=-1), B_w1, B_b1, B_w2, B_b2)
    return h2
```

```cpp
#include <hip/hip_runtime.h>
#include <hip/hip_bf16.h>
#include <stdint.h>

typedef unsigned v4u __attribute__((ext_vector_type(4)));
typedef float f32x4 __attribute__((ext_vector_type(4)));
typedef short bf16x8 __attribute__((ext_vector_type(8)));
typedef __bf16 bf16x2 __attribute__((ext_vector_type(2)));
#define GAS __attribute__((address_space(1)))

constexpr int B = 8, D = 512, H = 32, SK = 4096, DH = 128, W = 1024, F = 1376;

__device__ __forceinline__ float bf2f(unsigned short b) { return __builtin_bit_cast(float, (unsigned)b << 16); }
__device__ __forceinline__ unsigned short f2bf(float f) { return __builtin_bit_cast(unsigned short, (__bf16)f); }
__device__ __forceinline__ float rbf(float f) { return bf2f(f2bf(f)); }
__device__ __forceinline__ float lo16(unsigned u) { return __builtin_bit_cast(float, u << 16); }
__device__ __forceinline__ float hi16(unsigned u) { return __builtin_bit_cast(float, u & 0xffff0000u); }

__device__ __forceinline__ float wave_sum(float v) {
  for (int m = 32; m >= 1; m >>= 1) v += __shfl_xor(v, m);
  return v;
}
__device__ __forceinline__ float wave_max(float v) {
  for (int m = 32; m >= 1; m >>= 1) v = fmaxf(v, __shfl_xor(v, m));
  return v;
}

__device__ __forceinline__ void ln8(float (&v)[8]) {
  float s = 0.f;
  for (int j = 0; j < 8; ++j) s += v[j];
  s = wave_sum(s);
  const float mean = s * (1.f / D);
  float q = 0.f;
  for (int j = 0; j < 8; ++j) { const float d = v[j] - mean; q += d * d; }
  q = wave_sum(q);
  const float var = rbf(q * (1.f / D));
  const float mb = rbf(mean);
  const float sd = rbf(sqrtf(rbf(var + 1e-5f)));
  for (int j = 0; j < 8; ++j) v[j] = rbf(rbf(v[j] - mb) / sd);
}

__global__ void __launch_bounds__(512, 2) k_attn(const unsigned short* x, const unsigned short* kc, const unsigned short* vc,
                                                 const unsigned short* wq, const unsigned short* wo, float* part) {
  __shared__ float xs[D];
  __shared__ float red[8][512];
  __shared__ float qs[DH];
  __shared__ float sc[W];
  __shared__ float ml[16];
  const int bid = blockIdx.x, xcd = bid & 7, k = bid >> 3, h = xcd * 4 + (k & 3), b = k >> 2;
  const int tid = threadIdx.x, lane = tid & 63, w = tid >> 6, r = lane >> 4, c = lane & 15;
  {
    v4u xv = *(const v4u*)(x + b * D + lane * 8);
    float v[8];
    for (int j = 0; j < 4; ++j) { v[2 * j] = lo16(xv[j]); v[2 * j + 1] = hi16(xv[j]); }
    ln8(v);
    if (w == 0) for (int j = 0; j < 8; ++j) xs[lane * 8 + j] = v[j];
  }
  __syncthreads();
  {
    float acc[8] = {0, 0, 0, 0, 0, 0, 0, 0};
#pragma unroll
    for (int i = 0; i < 16; ++i) {
      const int row = w * 64 + i * 4 + r;
      v4u wv = *(const v4u*)(wq + (size_t)row * (H * DH) + h * DH + c * 8);
      const float xv = xs[row];
      for (int j = 0; j < 4; ++j) { acc[2 * j] += xv * lo16(wv[j]); acc[2 * j + 1] += xv * hi16(wv[j]); }
    }
    for (int j = 0; j < 8; ++j) { acc[j] += __shfl_xor(acc[j], 16); acc[j] += __shfl_xor(acc[j], 32); }
    if (r == 0) for (int j = 0; j < 8; ++j) red[w][c * 8 + j] = acc[j];
  }
  __syncthreads();
  if (tid < DH) { float s = 0.f; for (int ww = 0; ww < 8; ++ww) s += red[ww][tid]; qs[tid] = rbf(s); }
  __syncthreads();
  float q[8];
  for (int j = 0; j < 8; ++j) q[j] = qs[c * 8 + j];
  const size_t base = ((size_t)(b * H + h) * SK + (SK - W) + w * 128) * DH;
#pragma unroll 8
  for (int i = 0; i < 32; ++i) {
    v4u kv = __builtin_nontemporal_load((const v4u*)(kc + base + i * 512 + lane * 8));
    float s = 0.f;
    for (int j = 0; j < 4; ++j) { s += q[2 * j] * lo16(kv[j]); s += q[2 * j + 1] * hi16(kv[j]); }
    s += __shfl_xor(s, 1); s += __shfl_xor(s, 2); s += __shfl_xor(s, 4); s += __shfl_xor(s, 8);
    if (c == 0) sc[w * 128 + i * 4 + r] = rbf(rbf(s) * 0.08838834764831845f);
  }
  __syncthreads();
  float s0 = sc[w * 128 + lane], s1 = sc[w * 128 + 64 + lane];
  float m = wave_max(fmaxf(s0, s1));
  s0 = __expf(s0 - m); s1 = __expf(s1 - m);
  float l = wave_sum(s0 + s1);
  sc[w * 128 + lane] = s0; sc[w * 128 + 64 + lane] = s1;
  if (lane == 0) { ml[w] = m; ml[8 + w] = l; }
  __syncthreads();
  float acc[8] = {0, 0, 0, 0, 0, 0, 0, 0};
#pragma unroll 8
  for (int i = 0; i < 32; ++i) {
    v4u vv = __builtin_nontemporal_load((const v4u*)(vc + base + i * 512 + lane * 8));
    const float p = sc[w * 128 + i * 4 + r];
    for (int j = 0; j < 4; ++j) { acc[2 * j] += p * lo16(vv[j]); acc[2 * j + 1] += p * hi16(vv[j]); }
  }
  for (int j = 0; j < 8; ++j) { acc[j] += __shfl_xor(acc[j], 16); acc[j] += __shfl_xor(acc[j], 32); }
  if (r == 0) for (int j = 0; j < 8; ++j) red[w][c * 8 + j] = acc[j];
  __syncthreads();
  if (tid < DH) {
    float M = ml[0];
    for (int ww = 1; ww < 8; ++ww) M = fmaxf(M, ml[ww]);
    float L = 0.f, o = 0.f;
    for (int ww = 0; ww < 8; ++ww) { const float e = __expf(ml[ww] - M); L += e * ml[8 + ww]; o += e * red[ww][tid]; }
    qs[tid] = rbf(o / L);
  }
  __syncthreads();
  {
    float a2[8] = {0, 0, 0, 0, 0, 0, 0, 0};
#pragma unroll
    for (int i = 0; i < 16; ++i) {
      const int row = w * 16 + i;
      v4u wv = *(const v4u*)(wo + (size_t)(h * DH + row) * D + lane * 8);
      const float ov = qs[row];
      for (int j = 0; j < 4; ++j) { a2[2 * j] += ov * lo16(wv[j]); a2[2 * j + 1] += ov * hi16(wv[j]); }
    }
    for (int j = 0; j < 8; ++j) red[w][lane * 8 + j] = a2[j];
  }
  __syncthreads();
  { float s = 0.f; for (int ww = 0; ww < 8; ++ww) s += red[ww][tid]; part[(size_t)(b * H + h) * D + tid] = s; }
}

__global__ void __launch_bounds__(512, 2) k_mlp(const unsigned short* x, const float* part, const unsigned short* w1, const unsigned short* b1,
                                                const unsigned short* w2, float* hbuf, float* part2) {
  __shared__ float hs[D];
  __shared__ float xs[D];
  __shared__ float red[8][64];
  __shared__ float g[64];
  const int bid = blockIdx.x, xcd = bid & 7, k = bid >> 3, j0 = xcd * 4 + (k & 3), b = k >> 2;
  const int tid = threadIdx.x, lane = tid & 63, w = tid >> 6;
  float s = 0.f;
#pragma unroll
  for (int hh = 0; hh < H; ++hh) s += part[(size_t)(b * H + hh) * D + tid];
  const float hv = rbf(bf2f(x[b * D + tid]) + rbf(s));
  hs[tid] = hv;
  if (j0 == 0) hbuf[b * D + tid] = hv;
  __syncthreads();
  {
    float v[8];
    for (int j = 0; j < 8; ++j) v[j] = hs[lane * 8 + j];
    ln8(v);
    if (w == 0) for (int j = 0; j < 8; ++j) xs[lane * 8 + j] = v[j];
  }
  __syncthreads();
  float a = 0.f;
  if (lane < 43) {
#pragma unroll 16
    for (int i = 0; i < 64; ++i) { const int row = w * 64 + i; a += xs[row] * bf2f(w1[(size_t)row * F + j0 * 43 + lane]); }
  }
  red[w][lane] = a;
  __syncthreads();
  if (tid < 43) {
    float u = 0.f;
    for (int ww = 0; ww < 8; ++ww) u += red[ww][tid];
    u = rbf(rbf(u) + bf2f(b1[j0 * 43 + tid]));
    const float t = 0.7978845608028654f * (u + 0.044715f * u * u * u);
    g[tid] = rbf(0.5f * u * (1.f + tanhf(t)));
  }
  __syncthreads();
  float o = 0.f;
#pragma unroll
  for (int i = 0; i < 43; ++i) o += g[i] * bf2f(w2[(size_t)(j0 * 43 + i) * D + tid]);
  part2[(size_t)(b * H + j0) * D + tid] = o;
}

__global__ void __launch_bounds__(512) k_fin(const float* hbuf, const float* part2, const unsigned short* b2, unsigned short* out) {
  const int b = blockIdx.x, tid = threadIdx.x;
  float s = 0.f;
#pragma unroll
  for (int hh = 0; hh < H; ++hh) s += part2[(size_t)(b * H + hh) * D + tid];
  out[b * D + tid] = f2bf(hbuf[b * D + tid] + rbf(rbf(s) + bf2f(b2[tid])));
}

extern "C" void kernel_launch(void* const* d_in, const int* in_sizes, int n_in, void* d_out, int out_size, void* d_ws, size_t ws_size,
                              hipStream_t stream) {
  const unsigned short* x = (const unsigned short*)d_in[0];
  const unsigned short* kc = (const unsigned short*)d_in[1];
  const unsigned short* vc = (const unsigned short*)d_in[2];
  const unsigned short* wq = (const unsigned short*)d_in[3];
  const unsigned short* wo = (const unsigned short*)d_in[4];
  const unsigned short* w1 = (const unsigned short*)d_in[6];
  const unsigned short* b1 = (const unsigned short*)d_in[7];
  const unsigned short* w2 = (const unsigned short*)d_in[8];
  const unsigned short* b2 = (const unsigned short*)d_in[9];
  float* part = (float*)d_ws;
  float* part2 = part + 256 * 512;
  float* hbuf = part2 + 256 * 512;
  k_attn<<<256, 512, 0, stream>>>(x, kc, vc, wq, wo, part);
  k_mlp<<<256, 512, 0, stream>>>(x, part, w1, b1, w2, hbuf, part2);
  k_fin<<<8, 512, 0, stream>>>(hbuf, part2, b2, (unsigned short*)d_out);
}
```

```cpp
#define STAMPS 1
#define PROBE 0
#include <hip/hip_runtime.h>
#include <hip/hip_bf16.h>
#include <stdint.h>

namespace {

typedef unsigned v4u __attribute__((ext_vector_type(4)));
typedef unsigned v2u __attribute__((ext_vector_type(2)));
typedef float f32x4 __attribute__((ext_vector_type(4)));
typedef float f32x2 __attribute__((ext_vector_type(2)));
typedef __bf16 bf16x8 __attribute__((ext_vector_type(8)));
typedef __bf16 bf16x2 __attribute__((ext_vector_type(2)));
typedef __amdgpu_buffer_rsrc_t rsrc_t;
#define LAS __attribute__((address_space(3)))

constexpr int NB = 8, D = 512, H = 32, SK = 4096, DH = 128, WIN_KEYS = 1024, F = 1376;

#define WIN 3
#define WQ0 4
#define WQPER 2
#define QBAR_LAG 1
#define WSTART 12
#define WEND 58
#define PACE 0
#define PACE_LEAD 2
#define ROTMUL 37
#define AUX_W 2
#define DLY1 0
#define DLY2 8
#define DLY3 12
constexpr int AUX_NT = 2, AUX_SC1 = 16;
constexpr int RS3 = 0x00020000;
constexpr unsigned SENT = 0xFFFFFFFFu;
constexpr unsigned SPIN_CAP = 1u << 18;

constexpr int WS_N = 0;
constexpr int WS_E = 4096;
constexpr int WS_B2 = WS_E + 32768;
constexpr int WS_B1 = 65536;
constexpr int WS_B3 = WS_B1 + 524288;
constexpr int WS_END = WS_B3 + 524288;

template <int I, int N, class Fn> __device__ __forceinline__ void static_for(Fn&& fn) {
  if constexpr (I < N) { fn(std::integral_constant<int, I>{}); static_for<I + 1, N>(fn); }
}
__device__ __forceinline__ float lo16(unsigned u) { return __builtin_bit_cast(float, u << 16); }
__device__ __forceinline__ float hi16(unsigned u) { return __builtin_bit_cast(float, u & 0xffff0000u); }
__device__ __forceinline__ unsigned pk2(float a, float b) {
  bf16x2 r = {(__bf16)a, (__bf16)b};
  return __builtin_bit_cast(unsigned, r);
}
__device__ __forceinline__ float rbf(float a) { return lo16(pk2(a, a)); }
template <int CTRL> __device__ __forceinline__ unsigned dppu(unsigned x) { return (unsigned)__builtin_amdgcn_mov_dpp((int)x, CTRL, 0xf, 0xf, true); }
template <int CTRL> __device__ __forceinline__ float dppf(float x) { return __builtin_bit_cast(float, dppu<CTRL>(__builtin_bit_cast(unsigned, x))); }
constexpr int XOR1 = 0xB1, XOR2 = 0x4E, HMIR = 0x141, ROR4 = 0x124, ROR8 = 0x128, BCAST = 0x150;
__device__ __forceinline__ float row_sum(float s) { s += dppf<XOR1>(s); s += dppf<XOR2>(s); s += dppf<HMIR>(s); s += dppf<ROR8>(s); return s; }
__device__ __forceinline__ float row_max(float s) { s = fmaxf(s, dppf<XOR1>(s)); s = fmaxf(s, dppf<XOR2>(s)); s = fmaxf(s, dppf<HMIR>(s)); s = fmaxf(s, dppf<ROR8>(s)); return s; }
__device__ __forceinline__ float xrow_sum(float x) {
  auto s = __builtin_amdgcn_permlane16_swap(__float_as_uint(x), __float_as_uint(x), false, false);
  x = __uint_as_float(s[0]) + __uint_as_float(s[1]);
  auto t = __builtin_amdgcn_permlane32_swap(__float_as_uint(x), __float_as_uint(x), false, false);
  return __uint_as_float(t[0]) + __uint_as_float(t[1]);
}
__device__ __forceinline__ float xrow_max(float x) {
  auto s = __builtin_amdgcn_permlane16_swap(__float_as_uint(x), __float_as_uint(x), false, false);
  x = fmaxf(__uint_as_float(s[0]), __uint_as_float(s[1]));
  auto t = __builtin_amdgcn_permlane32_swap(__float_as_uint(x), __float_as_uint(x), false, false);
  return fmaxf(__uint_as_float(t[0]), __uint_as_float(t[1]));
}
__device__ __forceinline__ f32x4 mfma(v4u a, v4u b, f32x4 c) {
  return __builtin_amdgcn_mfma_f32_16x16x32_bf16(__builtin_bit_cast(bf16x8, a), __builtin_bit_cast(bf16x8, b), c, 0, 0, 0);
}
__device__ __forceinline__ float dot2(unsigned k, unsigned q, float acc) {
  return __builtin_amdgcn_fdot2_f32_bf16(__builtin_bit_cast(bf16x2, k), __builtin_bit_cast(bf16x2, q), acc, false);
}
template <int I> __device__ __forceinline__ f32x4 mv4(unsigned dup, const v4u& msk, v4u w, f32x4 acc) {
  const unsigned bc = dppu<BCAST + I>(dup);
  const v4u a = {bc & msk.x, bc & msk.y, bc & msk.z, bc & msk.w};
  return mfma(a, w, acc);
}
template <int I> __device__ __forceinline__ f32x4 mv8(unsigned pair, const v4u& sel, v4u w, f32x4 acc) {
  const unsigned bc = dppu<BCAST + I>(pair);
  const v4u a = {__builtin_amdgcn_perm(bc, bc, sel.x), __builtin_amdgcn_perm(bc, bc, sel.y), __builtin_amdgcn_perm(bc, bc, sel.z), __builtin_amdgcn_perm(bc, bc, sel.w)};
  return mfma(a, w, acc);
}
__device__ __forceinline__ f32x4 quad_fold(f32x4 d) {
  f32x4 y;
#pragma unroll
  for (int t = 0; t < 4; ++t) {
    auto s = __builtin_amdgcn_permlane32_swap(__float_as_uint(d[t]), __float_as_uint(d[t]), false, false);
    y[t] = __uint_as_float(s[0]) + dppf<ROR8>(__uint_as_float(s[1]));
  }
  return y;
}
__device__ __forceinline__ unsigned nosent(unsigned u) { return u == SENT ? 0x7FC07FC0u : u; }
__device__ __forceinline__ v4u nosent4(f32x4 y) { return v4u{nosent(__float_as_uint(y.x)), nosent(__float_as_uint(y.y)), nosent(__float_as_uint(y.z)), nosent(__float_as_uint(y.w))}; }
__device__ __forceinline__ bool ok4(v4u v) { return v.x != SENT && v.y != SENT && v.z != SENT && v.w != SENT; }
__device__ __forceinline__ rsrc_t mk(const void* p, int bytes) { return __builtin_amdgcn_make_buffer_rsrc((void*)p, 0, bytes, RS3); }
__device__ __forceinline__ void lds_barrier() {
  asm volatile("s_waitcnt lgkmcnt(0)\n\ts_barrier" ::: "memory");
}
#define SCHED() __builtin_amdgcn_sched_barrier(0)

#define STAMP(i) do { if (w == 0) { tst[i] = __builtin_amdgcn_s_memrealtime(); } } while (0)

__device__ __forceinline__ void ln_stats(v4u xv, float& mean, float& rstd) {
  const float sh = lo16((unsigned)__builtin_amdgcn_readfirstlane((int)xv.x));
  float s = 0.f, q = 0.f;
#pragma unroll
  for (int j = 0; j < 4; ++j) {
    const float a = lo16(xv[j]) - sh, b = hi16(xv[j]) - sh;
    s += a + b; q = fmaf(a, a, fmaf(b, b, q));
  }
  s = xrow_sum(row_sum(s)); q = xrow_sum(row_sum(q));
  const float ms = s * (1.f / D);
  const float var = fmaxf(fmaf(-ms, ms, q * (1.f / D)), 0.f);
  mean = sh + ms;
  rstd = __builtin_amdgcn_rsqf(var + 1e-5f);
}

struct Lds {
  float qp[8][DH];
  float ml[8][2];
  unsigned hn[D / 2];
  float up[8][64];
  unsigned flag[4];
};

__global__ void __launch_bounds__(512, 2)
fused_decode(const unsigned short* __restrict__ x, const unsigned char* __restrict__ kc, const unsigned char* __restrict__ vc,
             const unsigned char* __restrict__ wq, const unsigned char* __restrict__ wo, const unsigned char* __restrict__ w1,
             const unsigned short* __restrict__ b1, const unsigned char* __restrict__ w2, const unsigned short* __restrict__ b2,
             unsigned short* __restrict__ out, unsigned char* ws) {
  __shared__ __attribute__((aligned(16))) Lds L;
  const unsigned t0 = PACE ? (unsigned)__builtin_amdgcn_s_memrealtime() : 0u;
  const int bid = blockIdx.x, kk = bid >> 3, h = (bid & 7) * 4 + (kk & 3), b = kk >> 2;
  const int tid = threadIdx.x, lane = tid & 63, w = __builtin_amdgcn_readfirstlane(tid >> 6), g = lane >> 4, c = lane & 15;
  unsigned long long tst[16];
#pragma unroll
  for (int i = 0; i < 16; ++i) tst[i] = 0;
  STAMP(0);
  const int u0 = (43 * h) >> 3, nu = ((43 * (h + 1)) >> 3) - u0;

  const size_t kvbase = ((size_t)(b * H + h) * SK + (SK - WIN_KEYS)) * (DH * 2);
  const rsrc_t rK = mk(kc + kvbase, WIN_KEYS * DH * 2), rV = mk(vc + kvbase, WIN_KEYS * DH * 2);
  const rsrc_t rWq = mk(wq, D * H * DH * 2), rWo = mk(wo + (size_t)h * DH * D * 2, DH * D * 2);
  const rsrc_t rW1 = mk(w1, D * F * 2), rW2 = mk(w2 + (size_t)u0 * 8 * D * 2, nu * 8 * D * 2);
  const rsrc_t rX = mk(x + b * D, D * 2), rWs = mk(ws, WS_END);
  const int rot = (bid * ROTMUL + w) & 255;
  const int voKV = lane * 16;
  const int voWq = (64 * w + 16 * g) * (H * DH * 2) + h * (DH * 2) + c * 16;
  const int voWo = (32 * g + (c >> 3)) * (D * 2) + w * 128 + (c & 7) * 16;
  const int voW1 = (c & 7) < nu ? (64 * w + 16 * g + (c >> 3)) * (F * 2) + (u0 + (c & 7)) * 16 : 0x7fff0000;
  const int voW2 = (12 * g + (c >> 3)) * (D * 2) + w * 128 + (c & 7) * 16;
  const bool publ = g < 2 && c < 8;
  const int voPub = ((b * 32 + 4 * w + (c >> 1)) * 32 + h) * 64 + (c & 1) * 32 + g * 16;

  v4u kv[64], wqr[16], wt[30];
  auto ldkv = [&](auto S_) { constexpr int s = S_; kv[s] = __builtin_amdgcn_raw_buffer_load_b128(s < 32 ? rK : rV, voKV, ((rot + 8 * (s & 31)) & 255) * 1024, AUX_NT); };
  auto ldwq = [&](auto I_) { constexpr int i = I_; wqr[i] = __builtin_amdgcn_raw_buffer_load_b128(rWq, voWq, i * (H * DH * 2), AUX_W); };
  auto ldwt = [&](auto I_) {
    constexpr int i = I_;
    if constexpr (i < 16) wt[i] = __builtin_amdgcn_raw_buffer_load_b128(rWo, voWo, i * (2 * D * 2), AUX_W);
    else if constexpr (i < 24) wt[i] = __builtin_amdgcn_raw_buffer_load_b128(rW1, voW1, (i - 16) * (2 * F * 2), AUX_W);
    else wt[i] = __builtin_amdgcn_raw_buffer_load_b128(rW2, voW2, (i - 24) * (2 * D * 2), AUX_W);
  };
  const unsigned ph = ((unsigned)(bid * 8 + w) * 79u) & 255u;
  auto pace = [&](int s) {
    if (PACE) {
      const unsigned due = (unsigned)((s * 256 - (int)ph) * PACE) >> 14;
      while ((unsigned)__builtin_amdgcn_s_memrealtime() - t0 < due) __builtin_amdgcn_s_sleep(1);
    }
  };

  const v4u xrow = __builtin_amdgcn_raw_buffer_load_b128(rX, lane * 16, 0, 0);
  const unsigned xe = __builtin_amdgcn_raw_buffer_load_b16(rX, (64 * w + lane) * 2, 0, 0);
  SCHED();
  static_for<0, (PACE && PACE_LEAD < WIN ? PACE_LEAD : WIN)>(ldkv);
  static_for<0, WQ0>(ldwq);
  SCHED();
  const v2u xres = __builtin_amdgcn_raw_buffer_load_b64(rX, (16 * h + 4 * (lane & 3)) * 2, 0, 0);
  const v2u b2v = __builtin_amdgcn_raw_buffer_load_b64(mk(b2, D * 2), (16 * h + 4 * (lane & 3)) * 2, 0, 0);
  const unsigned b1v = __builtin_amdgcn_raw_buffer_load_b32(mk(b1 + u0 * 8, nu * 16), (12 * g + 2 * c) * 2, 0, 0);
  unsigned snap = 0;
  if (w == 0 && lane < 32) snap = __builtin_amdgcn_raw_buffer_load_b32(rWs, lane == h ? WS_N + (b * 32 + h) * 4 : WS_E + ((b * 32 + h) * 32 + lane) * 4, 0, AUX_SC1);
  {
    const v4u sent = {SENT, SENT, SENT, SENT};
    if (publ) {
      __builtin_amdgcn_raw_buffer_store_b128(sent, rWs, voPub, WS_B1, AUX_SC1);
      __builtin_amdgcn_raw_buffer_store_b128(sent, rWs, voPub, WS_B3, AUX_SC1);
    }
    if (w == 0 && lane < 4) __builtin_amdgcn_raw_buffer_store_b64(v2u{SENT, SENT}, rWs, b * 1024 + h * 32 + lane * 8, WS_B2, AUX_SC1);
  }
  SCHED();
  if constexpr (PACE && PACE_LEAD < WIN) static_for<PACE_LEAD, WIN>([&](auto S_) { pace(S_ - PACE_LEAD + 1); ldkv(S_); SCHED(); });
  v4u msk, sel;
#pragma unroll
  for (int t = 0; t < 4; ++t) {
    msk[t] = (c >> 1) == t ? ((c & 1) ? 0xFFFF0000u : 0x0000FFFFu) : 0u;
    const unsigned pr = 0x0100u + (unsigned)(c >> 3) * 0x0202u;
    sel[t] = ((c & 7) >> 1) == t ? ((c & 1) ? (pr << 16) | 0x0c0cu : 0x0c0c0000u | pr) : 0x0c0c0c0cu;
  }
  unsigned xdup;
  {
    float mean, rstd;
    ln_stats(xrow, mean, rstd);
    const float xn = (lo16(xe) - mean) * rstd;
    xdup = pk2(xn, xn);
  }
  STAMP(1);
  SCHED();

  constexpr int SLOT_WQ_LAST = (16 - WQ0 + WQPER - 1) / WQPER - 1;
  constexpr int SLOT_QW = SLOT_WQ_LAST + WIN + 1;
  constexpr int SLOT_QB = SLOT_QW + QBAR_LAG;
  constexpr int SLOT_NONCE = 1, SLOT_ECHO_LD = SLOT_QB + 6, SLOT_ECHO_ST = SLOT_ECHO_LD + WIN + 1, SLOT_CHK_LD = 44, SLOT_CHK = SLOT_CHK_LD + WIN + 1;
  constexpr int SLOT_ML = 40;
  f32x4 qacc = {0.f, 0.f, 0.f, 0.f}, oacc = {0.f, 0.f, 0.f, 0.f};
  v4u qpk = {0u, 0u, 0u, 0u};
  float sc0 = 0.f, sc1 = 0.f, mloc = 0.f, oscale = 0.f;
  unsigned pp0 = 0u, pp1 = 0u, nonce = 0u, peerN = 0u, echo = 0u;
  bool ready = false;
  constexpr float C2 = 0.08838834764831845f * 1.4426950408889634f;
  auto score = [&](auto S_) {
    constexpr int s = S_;
    float d = dot2(kv[s].x, qpk.x, 0.f);
    d = dot2(kv[s].y, qpk.y, d); d = dot2(kv[s].z, qpk.z, d); d = dot2(kv[s].w, qpk.w, d);
    d = row_sum(d);
    if constexpr (s < 16) sc0 = c == s ? d : sc0; else sc1 = c == s - 16 ? d : sc1;
  };
  static_for<0, 64>([&](auto S_) {
    constexpr int s = S_;
    asm volatile("" ::"v"(kv[s]));
    if constexpr (s + WIN < 64) { pace(s + WIN - PACE_LEAD + 1); ldkv(std::integral_constant<int, s + WIN>{}); }
    static_for<WQ0 + s * WQPER, (WQ0 + (s + 1) * WQPER < 16 ? WQ0 + (s + 1) * WQPER : 16)>(ldwq);
    if constexpr (s >= WSTART && s < WEND) static_for<(s - WSTART) * 30 / (WEND - WSTART), (s + 1 - WSTART) * 30 / (WEND - WSTART)>(ldwt);
    if constexpr (s == SLOT_NONCE) {
      if (w == 0) {
        nonce = (unsigned)__builtin_amdgcn_ballot_w64(((snap >> (lane & 31)) & 1u) == 0u);
        if (lane == 0) __builtin_amdgcn_raw_buffer_store_b32(nonce, rWs, WS_N + (b * 32 + h) * 4, 0, AUX_SC1);
      }
    }
    if constexpr (s == SLOT_ECHO_LD) { if (w == 0 && lane < 32) peerN = __builtin_amdgcn_raw_buffer_load_b32(rWs, WS_N + (b * 32 + lane) * 4, 0, AUX_SC1); }
    if constexpr (s == SLOT_ECHO_ST) { if (w == 0 && lane < 32) __builtin_amdgcn_raw_buffer_store_b32(peerN, rWs, WS_E + ((b * 32 + lane) * 32 + h) * 4, 0, AUX_SC1); }
    if constexpr (s == SLOT_CHK_LD) { if (w == 0 && lane < 32) echo = __builtin_amdgcn_raw_buffer_load_b32(rWs, WS_E + ((b * 32 + h) * 32 + lane) * 4, 0, AUX_SC1); }
    if constexpr (s == SLOT_CHK) { if (w == 0) ready = __all(lane >= 32 || lane == h || echo == nonce); }
    SCHED();
    if constexpr (s <= SLOT_QW) {
      constexpr int lo = s <= WIN ? 0 : WQ0 + (s - WIN - 1) * WQPER, hi = s < WIN ? 0 : WQ0 + (s - WIN) * WQPER;
      static_for<(lo < 16 ? lo : 16), (hi < 16 ? hi : 16)>([&](auto I_) { qacc = mv4<I_>(xdup, msk, wqr[I_], qacc); });
    }
    if constexpr (s == SLOT_QW) { if (g < 2) *(LAS f32x4*)&L.qp[w][8 * c + 4 * g] = qacc; }
    if constexpr (s == SLOT_QB) {
      lds_barrier();
      f32x4 a = *(LAS f32x4*)&L.qp[0][8 * c], bb = *(LAS f32x4*)&L.qp[0][8 * c + 4];
#pragma unroll
      for (int ww = 1; ww < 8; ++ww) { a += *(LAS f32x4*)&L.qp[ww][8 * c]; bb += *(LAS f32x4*)&L.qp[ww][8 * c + 4]; }
      qpk = v4u{pk2(a.x, a.y), pk2(a.z, a.w), pk2(bb.x, bb.y), pk2(bb.z, bb.w)};
      STAMP(2);
    }
    if constexpr (s >= SLOT_QB && s < 32) {
      constexpr int lo = 3 * (s - SLOT_QB) < s ? 3 * (s - SLOT_QB) : s, hi = 3 * (s + 1 - SLOT_QB) < s + 1 ? 3 * (s + 1 - SLOT_QB) : s + 1;
      static_for<lo, hi>(score);
    }
    if constexpr (s == 31) {
      static_assert(3 * (32 - SLOT_QB) >= 32, "scores must have caught up");
      mloc = xrow_max(row_max(fmaxf(sc0, sc1))) * C2;
      const float p0 = __builtin_amdgcn_exp2f(fmaf(sc0, C2, -mloc)), p1 = __builtin_amdgcn_exp2f(fmaf(sc1, C2, -mloc));
      const float l = xrow_sum(row_sum(p0 + p1));
      pp0 = pk2(p0, p0); pp1 = pk2(p1, p1);
      if (lane == 0) *(LAS f32x2*)&L.ml[w][0] = f32x2{mloc, l};
      STAMP(3);
    }
    if constexpr (s >= 32) oacc = mv4<(s & 15)>(s < 48 ? pp0 : pp1, msk, kv[s], oacc);
    if constexpr (s == SLOT_ML) {
      lds_barrier();
      const f32x2 e = *(LAS f32x2*)&L.ml[lane & 7][0];
      float M = e.x;
      M = fmaxf(M, dppf<XOR1>(M)); M = fmaxf(M, dppf<XOR2>(M)); M = fmaxf(M, dppf<HMIR>(M));
      float t = e.y * __builtin_amdgcn_exp2f(e.x - M);
      t += dppf<XOR1>(t); t += dppf<XOR2>(t); t += dppf<HMIR>(t);
      oscale = __builtin_amdgcn_exp2f(mloc - M) / t;
    }
    SCHED();
  });
  STAMP(4);

  if (g < 2) *(LAS f32x4*)&L.qp[w][8 * c + 4 * g] = oacc * oscale;
  lds_barrier();
  {
    f32x2 o = *(LAS f32x2*)&L.qp[0][2 * lane];
#pragma unroll
    for (int ww = 1; ww < 8; ++ww) o += *(LAS f32x2*)&L.qp[ww][2 * lane];
    const unsigned opair = pk2(o.x, o.y);
    f32x4 y0 = {0.f, 0.f, 0.f, 0.f}, y1 = {0.f, 0.f, 0.f, 0.f};
    static_for<0, 8>([&](auto I_) { y0 = mv8<2 * I_>(opair, sel, wt[2 * I_], y0); y1 = mv8<2 * I_ + 1>(opair, sel, wt[2 * I_ + 1], y1); });
    const f32x4 y = quad_fold(y0 + y1);
    if (publ) __builtin_amdgcn_raw_buffer_store_b128(nosent4(y), rWs, voPub, WS_B1, AUX_SC1);
  }
  STAMP(5);
  f32x4 hres = {0.f, 0.f, 0.f, 0.f};
  auto re_echo = [&]() {
    if (lane < 32) {
      const unsigned n = __builtin_amdgcn_raw_buffer_load_b32(rWs, WS_N + (b * 32 + lane) * 4, 0, AUX_SC1);
      __builtin_amdgcn_raw_buffer_store_b32(n, rWs, WS_E + ((b * 32 + lane) * 32 + h) * 4, 0, AUX_SC1);
    }
  };
  auto gather32 = [&](int box, bool echoing) -> f32x4 {
    v4u a0, a1;
    for (unsigned spins = 1;; ++spins) {
      asm volatile("" ::: "memory");
      a0 = __builtin_amdgcn_raw_buffer_load_b128(rWs, (b * 32 + h) * 2048 + lane * 16, box, AUX_SC1);
      a1 = __builtin_amdgcn_raw_buffer_load_b128(rWs, (b * 32 + h) * 2048 + 1024 + lane * 16, box, AUX_SC1);
      if (__all(ok4(a0) && ok4(a1)) || spins > SPIN_CAP) break;
      if (echoing && (spins & 15u) == 0u) re_echo();
    }
    f32x4 s = __builtin_bit_cast(f32x4, a0) + __builtin_bit_cast(f32x4, a1);
#pragma unroll
    for (int t = 0; t < 4; ++t) { float v = s[t]; v += dppf<ROR4>(v); v += dppf<ROR8>(v); s[t] = xrow_sum(v); }
    return s;
  };
  if (w == 0) {
    for (unsigned spins = 1; !ready && spins <= SPIN_CAP; ++spins) {
      asm volatile("" ::: "memory");
      re_echo();
      if (lane < 32) echo = __builtin_amdgcn_raw_buffer_load_b32(rWs, WS_E + ((b * 32 + h) * 32 + lane) * 4, 0, AUX_SC1);
      ready = __all(lane >= 32 || lane == h || echo == nonce);
    }
    if (DLY1) __builtin_amdgcn_s_sleep(DLY1);
    const f32x4 s = gather32(WS_B1, true);
    STAMP(6);
    const unsigned h01 = pk2(lo16(xres.x) + s.x, hi16(xres.x) + s.y), h23 = pk2(lo16(xres.y) + s.z, hi16(xres.y) + s.w);
    if (lane < 4) __builtin_amdgcn_raw_buffer_store_b64(v2u{nosent(h01), nosent(h23)}, rWs, b * 1024 + h * 32 + lane * 8, WS_B2, AUX_SC1);
    hres = f32x4{lo16(h01) + lo16(b2v.x), hi16(h01) + hi16(b2v.x), lo16(h23) + lo16(b2v.y), hi16(h23) + hi16(b2v.y)};
    if (DLY2) __builtin_amdgcn_s_sleep(DLY2);
    v4u hv;
    for (unsigned spins = 1;; ++spins) {
      asm volatile("" ::: "memory");
      hv = __builtin_amdgcn_raw_buffer_load_b128(rWs, b * 1024 + lane * 16, WS_B2, AUX_SC1);
      if (__all(ok4(hv)) || spins > SPIN_CAP) break;
      if ((spins & 15u) == 0u) re_echo();
    }
    STAMP(7);
    float mean, rstd;
    ln_stats(hv, mean, rstd);
    v4u hn;
#pragma unroll
    for (int j = 0; j < 4; ++j) hn[j] = pk2((lo16(hv[j]) - mean) * rstd, (hi16(hv[j]) - mean) * rstd);
    *(LAS v4u*)&L.hn[4 * lane] = hn;
  }
  lds_barrier();
  STAMP(8);
  {
    const unsigned hp = L.hn[32 * w + 8 * g + (c & 7)];
    f32x4 y0 = {0.f, 0.f, 0.f, 0.f}, y1 = {0.f, 0.f, 0.f, 0.f};
    static_for<0, 4>([&](auto I_) { y0 = mv8<2 * I_>(hp, sel, wt[16 + 2 * I_], y0); y1 = mv8<2 * I_ + 1>(hp, sel, wt[16 + 2 * I_ + 1], y1); });
    const f32x4 y = quad_fold(y0 + y1);
    if (publ) *(LAS f32x4*)&L.up[w][8 * c + 4 * g] = y;
  }
  lds_barrier();
  STAMP(9);
  {
    const int idx = (12 * g + 2 * c) & 63;
    f32x2 u = *(LAS f32x2*)&L.up[0][idx];
#pragma unroll
    for (int ww = 1; ww < 8; ++ww) u += *(LAS f32x2*)&L.up[ww][idx];
    u.x += lo16(b1v); u.y += hi16(b1v);
    constexpr float G1 = -2.f * 0.7978845608028654f * 1.4426950408889634f, G3 = G1 * 0.044715f;
    const float e0 = __builtin_amdgcn_exp2f(u.x * fmaf(u.x * u.x, G3, G1)), e1 = __builtin_amdgcn_exp2f(u.y * fmaf(u.y * u.y, G3, G1));
    const unsigned gp = pk2(u.x * __builtin_amdgcn_rcpf(1.f + e0), u.y * __builtin_amdgcn_rcpf(1.f + e1));
    f32x4 y0 = {0.f, 0.f, 0.f, 0.f}, y1 = {0.f, 0.f, 0.f, 0.f};
    static_for<0, 3>([&](auto I_) { y0 = mv8<2 * I_>(gp, sel, wt[24 + 2 * I_], y0); y1 = mv8<2 * I_ + 1>(gp, sel, wt[24 + 2 * I_ + 1], y1); });
    const f32x4 y = quad_fold(y0 + y1);
    if (publ) __builtin_amdgcn_raw_buffer_store_b128(nosent4(y), rWs, voPub, WS_B3, AUX_SC1);
  }
  STAMP(10);
  if (w == 0) {
    if (DLY3) __builtin_amdgcn_s_sleep(DLY3);
    const f32x4 s = gather32(WS_B3, false) + hres;
    if (lane < 4) *(v2u*)(out + b * D + 16 * h + 4 * lane) = v2u{pk2(s.x, s.y), pk2(s.z, s.w)};
    STAMP(11);
    { const unsigned long long tx = tst[0] + 5000 + (tst[PROBE] - tst[0]);
      while (__builtin_amdgcn_s_memrealtime() < tx) __builtin_amdgcn_s_sleep(1); }
  }
}

}

extern "C" void kernel_launch(void* const* d_in, const int* in_sizes, int n_in, void* d_out, int out_size, void* d_ws, size_t ws_size,
                              hipStream_t stream) {
  fused_decode<<<256, 512, 0, stream>>>((const unsigned short*)d_in[0], (const unsigned char*)d_in[1], (const unsigned char*)d_in[2],
                                        (const unsigned char*)d_in[3], (const unsigned char*)d_in[4], (const unsigned char*)d_in[6],
                                        (const unsigned short*)d_in[7], (const unsigned char*)d_in[8], (const unsigned short*)d_in[9],
                                        (unsigned short*)d_out, (unsigned char*)d_ws);
}
```

```cpp
#include <hip/hip_runtime.h>
#include <hip/hip_bf16.h>
#include <stdint.h>

namespace {

typedef unsigned v8u __attribute__((ext_vector_type(8)));
typedef unsigned v4u __attribute__((ext_vector_type(4)));
typedef unsigned v2u __attribute__((ext_vector_type(2)));
typedef float f32x4 __attribute__((ext_vector_type(4)));
typedef float f32x2 __attribute__((ext_vector_type(2)));
typedef __bf16 bf16x16 __attribute__((ext_vector_type(16)));
typedef __bf16 bf16x8 __attribute__((ext_vector_type(8)));
typedef __bf16 bf16x2 __attribute__((ext_vector_type(2)));
typedef __amdgpu_buffer_rsrc_t rsrc_t;
#define LAS __attribute__((address_space(3)))

constexpr int D = 512, H = 32, SK = 4096, DH = 128, WIN_KEYS = 1024, F = 1376;

#define KNOB(n, v) constexpr int n = v
#define WIN 7
#define PACE 1740
#define PFDT 12
#define K0 140
#define QSLOT 75
#define NPARK 36
#define CATCH 3
#define PRIO_W 0
#define XWIN 5
#define X0 170
#define XQ 34
#define XO 24
#define XSTAG 7
#define SHD 20
#define AUX_W 2
#define AUX_F 0
#define AUX_M 0
#define AUX_S 16
#define DLY1 16
#define DLY2 0
#define DLY3 24
#define DLYU 0
#define GAPK 0
#define NAPK 0
#define ALIGNK 112
#define SW 0
#define SNAPAT 0
#define SNAPDT 24
#define MWIN 3
#define VGAP 8
#define XPAT 28
#define XPAUSE 0
#define NCOPY 4
#define AUX_O 0
#define DLY2B 2
#define NW2A 0
#define S2 72
#define PACE2 1700
#define NOSNAP 0
#define XQL 30
#define XOL 28
#define ZMASK 0
#define MPOL ""
constexpr int AUX_NT = 2, AUX_SC1 = 16;
constexpr int RS3 = 0x00020000;
constexpr int OOB = 0x40000000;
constexpr unsigned SENT = 0xFFFFFFFFu;
constexpr unsigned SPIN_CAP = 1u << 18;

constexpr int WS_B2 = 65536 + 524288;
constexpr int WS_DN = 16384;
constexpr int WS_UP = 32768;
constexpr int WS_B1 = 65536;
constexpr int WS_END = WS_B2 + 8 * NCOPY * 2048;

constexpr int piece_off(int s) { return (s & 1) << 13 | (s & 2) << 10 | (s & 4) << 10 | (s & 8) << 7 | (s & 48) << 12; }
template <int I, int N, class Fn> __device__ __forceinline__ void static_for(Fn&& fn) {
  if constexpr (I < N) { fn(std::integral_constant<int, I>{}); static_for<I + 1, N>(fn); }
}
__device__ __forceinline__ float lo16(unsigned u) { return __builtin_bit_cast(float, u << 16); }
__device__ __forceinline__ float hi16(unsigned u) { return __builtin_bit_cast(float, u & 0xffff0000u); }
__device__ __forceinline__ unsigned pk2(float a, float b) {
  bf16x2 r = {(__bf16)a, (__bf16)b};
  return __builtin_bit_cast(unsigned, r);
}
__device__ __forceinline__ unsigned hi_bf(float a) {
  unsigned r;
  asm("v_cvt_pk_bf16_f32 %0, 0, %1" : "=v"(r) : "v"(a));
  return r;
}
__device__ __forceinline__ float rbf(float a) { return hi16(pk2(a, a)); }
template <int CTRL> __device__ __forceinline__ unsigned dppu(unsigned x) { return (unsigned)__builtin_amdgcn_mov_dpp((int)x, CTRL, 0xf, 0xf, true); }
template <int CTRL> __device__ __forceinline__ float dppf(float x) { return __builtin_bit_cast(float, dppu<CTRL>(__builtin_bit_cast(unsigned, x))); }
constexpr int XOR1 = 0xB1, XOR2 = 0x4E, HMIR = 0x141, ROR4 = 0x124, ROR8 = 0x128, BCAST = 0x150;
__device__ __forceinline__ float row_sum(float s) { s += dppf<XOR1>(s); s += dppf<XOR2>(s); s += dppf<HMIR>(s); s += dppf<ROR8>(s); return s; }
__device__ __forceinline__ float row_max(float s) { s = fmaxf(s, dppf<XOR1>(s)); s = fmaxf(s, dppf<XOR2>(s)); s = fmaxf(s, dppf<HMIR>(s)); s = fmaxf(s, dppf<ROR8>(s)); return s; }
__device__ __forceinline__ float swap16_add(float a, float b) {
  auto s = __builtin_amdgcn_permlane16_swap(__float_as_uint(a), __float_as_uint(b), false, false);
  return __uint_as_float(s[0]) + __uint_as_float(s[1]);
}
__device__ __forceinline__ float swap32_add(float a, float b) {
  auto s = __builtin_amdgcn_permlane32_swap(__float_as_uint(a), __float_as_uint(b), false, false);
  return __uint_as_float(s[0]) + __uint_as_float(s[1]);
}
__device__ __forceinline__ float xrow_sum(float x) { x = swap16_add(x, x); return swap32_add(x, x); }
__device__ __forceinline__ float xrow_max(float x) {
  auto s = __builtin_amdgcn_permlane16_swap(__float_as_uint(x), __float_as_uint(x), false, false);
  x = fmaxf(__uint_as_float(s[0]), __uint_as_float(s[1]));
  auto t = __builtin_amdgcn_permlane32_swap(__float_as_uint(x), __float_as_uint(x), false, false);
  return fmaxf(__uint_as_float(t[0]), __uint_as_float(t[1]));
}
__device__ __forceinline__ float dot2(unsigned k, unsigned q, float acc) {
  return __builtin_amdgcn_fdot2_f32_bf16(__builtin_bit_cast(bf16x2, k), __builtin_bit_cast(bf16x2, q), acc, false);
}
__device__ __forceinline__ float score4(v4u k0, v4u k1, v4u k2, v4u k3, v4u q) {
  float x0, x1, x2, x3, y;
  asm("v_dot2_f32_bf16 %1, %5, %21, 0\n\tv_dot2_f32_bf16 %2, %9, %21, 0\n\tv_dot2_f32_bf16 %3, %13, %21, 0\n\tv_dot2_f32_bf16 %4, %17, %21, 0\n\t"
      "v_dot2c_f32_bf16 %1, %6, %22\n\tv_dot2c_f32_bf16 %2, %10, %22\n\tv_dot2c_f32_bf16 %3, %14, %22\n\tv_dot2c_f32_bf16 %4, %18, %22\n\t"
      "v_dot2c_f32_bf16 %1, %7, %23\n\tv_dot2c_f32_bf16 %2, %11, %23\n\tv_dot2c_f32_bf16 %3, %15, %23\n\tv_dot2c_f32_bf16 %4, %19, %23\n\t"
      "v_dot2c_f32_bf16 %1, %8, %24\n\tv_dot2c_f32_bf16 %2, %12, %24\n\tv_dot2c_f32_bf16 %3, %16, %24\n\tv_dot2c_f32_bf16 %4, %20, %24\n\t"
      "v_add_f32_dpp %1, %1, %1 row_ror:8 row_mask:0xf bank_mask:0x3\n\t"
      "v_add_f32_dpp %2, %2, %2 row_ror:8 row_mask:0xf bank_mask:0x3\n\t"
      "v_add_f32_dpp %1, %3, %3 row_ror:8 row_mask:0xf bank_mask:0xc\n\t"
      "v_add_f32_dpp %2, %4, %4 row_ror:8 row_mask:0xf bank_mask:0xc\n\t"
      "s_nop 0\n\t"
      "v_add_f32_dpp %0, %1, %1 row_half_mirror row_mask:0xf bank_mask:0x5\n\t"
      "v_add_f32_dpp %0, %2, %2 row_half_mirror row_mask:0xf bank_mask:0xa\n\t"
      "s_nop 1"
      : "=&v"(y), "=&v"(x0), "=&v"(x1), "=&v"(x2), "=&v"(x3)
      : "v"(k0.x), "v"(k0.y), "v"(k0.z), "v"(k0.w), "v"(k1.x), "v"(k1.y), "v"(k1.z), "v"(k1.w), "v"(k2.x), "v"(k2.y), "v"(k2.z), "v"(k2.w),
        "v"(k3.x), "v"(k3.y), "v"(k3.z), "v"(k3.w), "v"(q.x), "v"(q.y), "v"(q.z), "v"(q.w));
  return y;
}
template <int I> __device__ __forceinline__ f32x4 mv4(unsigned dup, const v4u& msk, v4u w, f32x4 acc) {
  const unsigned bc = dppu<BCAST + I>(dup);
  const v4u a = {bc & msk.x, bc & msk.y, bc & msk.z, bc & msk.w};
  return __builtin_amdgcn_mfma_f32_16x16x32_bf16(__builtin_bit_cast(bf16x8, a), __builtin_bit_cast(bf16x8, w), acc, 0, 0, 0);
}
__device__ __forceinline__ f32x4 smv(v4u a, v8u w, int idx, f32x4 acc) {
  return __builtin_amdgcn_smfmac_f32_16x16x64_bf16(__builtin_bit_cast(bf16x8, a), __builtin_bit_cast(bf16x16, w), acc, idx, 0, 0);
}
struct Tile { v8u lo, hi; };
__device__ __forceinline__ Tile mk_tile(v4u a, v4u b, v4u c, v4u d) {
  Tile t;
  t.lo = v8u{a.x, a.y, b.x, b.y, c.x, c.y, d.x, d.y};
  t.hi = v8u{a.z, a.w, b.z, b.w, c.z, c.w, d.z, d.w};
  asm volatile("" : "+v"(t.lo), "+v"(t.hi));
  return t;
}
__device__ __forceinline__ bool any(bool p) { return __builtin_amdgcn_ballot_w64(p) != 0ull; }
__device__ __forceinline__ rsrc_t mk(const void* p, int bytes) { return __builtin_amdgcn_make_buffer_rsrc((void*)p, 0, bytes, RS3); }
__device__ __forceinline__ void lds_barrier() { asm volatile("s_waitcnt lgkmcnt(0)\n\ts_barrier" ::: "memory"); }
#define DRAIN() asm volatile("s_waitcnt vmcnt(0)" ::: "memory")
#define SCHED() __builtin_amdgcn_sched_barrier(0)
__device__ __forceinline__ void lds_signal(LAS unsigned* cnt, int lane) {
  asm volatile("s_waitcnt lgkmcnt(0)" ::: "memory");
  if (lane == 0) __hip_atomic_fetch_add(cnt, 1u, __ATOMIC_RELAXED, __HIP_MEMORY_SCOPE_WORKGROUP);
}
__device__ __forceinline__ void lds_wait(LAS unsigned* cnt, unsigned want) {
  while ((unsigned)__builtin_amdgcn_readfirstlane((int)__hip_atomic_load(cnt, __ATOMIC_RELAXED, __HIP_MEMORY_SCOPE_WORKGROUP)) < want) __builtin_amdgcn_s_sleep(1);
  asm volatile("" ::: "memory");
}
template <int T, int NAP> __device__ __forceinline__ void wait_tick(unsigned base) {
  asm volatile("L_tick_%=:\n\ts_memrealtime vcc\n\ts_waitcnt lgkmcnt(0)\n\ts_sub_u32 vcc_lo, vcc_lo, %0\n\ts_cmp_ge_i32 vcc_lo, %1\n\t"
               "s_cbranch_scc1 L_tock_%=\n\ts_sleep %2\n\ts_branch L_tick_%=\nL_tock_%=:" ::"s"(base), "n"(T), "n"(NAP) : "vcc", "scc", "memory");
}
template <int T, int C, int DEC> __device__ __forceinline__ void wait_gap(unsigned base, int& slip) {
  if constexpr (C == 0) wait_tick<T, 1>(base);
  else
    asm volatile("L_tick_%=:\n\ts_memrealtime vcc\n\ts_waitcnt lgkmcnt(0)\n\ts_sub_u32 vcc_lo, vcc_lo, %1\n\ts_sub_u32 vcc_lo, vcc_lo, %2\n\ts_cmp_ge_i32 vcc_lo, %0\n\t"
                 "s_cbranch_scc1 L_tock_%=\n\ts_sleep 1\n\ts_branch L_tick_%=\nL_tock_%=:\n\ts_sub_u32 vcc_lo, vcc_lo, %3\n\ts_sub_u32 %0, %0, %4\n\ts_max_i32 %0, %0, 0\n\ts_max_i32 %0, %0, vcc_lo"
                 : "+s"(slip) : "s"(base), "n"(T), "n"(C), "n"(DEC) : "vcc", "scc", "memory");
}
#define NEW_CODE_PAGE() asm volatile("s_branch L_page_%=\n\t.p2align 12\nL_page_%=:" ::: "memory")
#define STAMP(i) do {} while (0)
#define PUT(i) do {} while (0)

struct Lds {
  unsigned ot[3][144];
  float up[9][128];
  float qp[4][DH];
  unsigned qb[DH / 2];
  v4u tag3[4][64];
  float ml[4][2];
  unsigned cnt[4];
  v4u park[4][NPARK][64];
};
constexpr int kv_tick(int s) { return ((s < S2 ? s * PACE : S2 * PACE + (s - S2) * PACE2) >> 7) + (s >= 64 ? VGAP : 0); }
constexpr int x_tick(int i) { return (i <= 28 ? i * XQ : 28 * XQ + (i - 28) * XO) + (i >= XPAT ? XPAUSE : 0); }
constexpr int wq_row(int i) { return 8 * (i & 15) + 16 * (i & 16); }
constexpr int wo_row(int i) { return 64 * ((i >> 1) & 1) + 8 * (i & 1) + 4 * ((i >> 2) & 1) + (i >> 3); }

__global__ void __launch_bounds__(512, 2)
fused_decode(const unsigned char* __restrict__ kc, const unsigned char* __restrict__ vc, unsigned char* ws, const unsigned short* __restrict__ x,
             const unsigned char* __restrict__ wq, const unsigned char* __restrict__ wo, const unsigned char* __restrict__ w1,
             const unsigned short* __restrict__ b1, const unsigned char* __restrict__ w2, const unsigned short* __restrict__ b2,
             unsigned short* __restrict__ out) {
  __shared__ __attribute__((aligned(16))) Lds L;
  const int bid = blockIdx.x, kk = bid >> 3, h = (bid & 7) * 4 + (kk & 3), b = kk >> 2, me = b * 32 + h;
  const int wv = __builtin_amdgcn_readfirstlane(threadIdx.x >> 6);
  LAS unsigned* cnt = (LAS unsigned*)L.cnt;
  int voWq = ((threadIdx.x >> 4) & 3) * (H * DH * 2) + (threadIdx.x & 15) * 16;
  int soWq = (4 * (wv & 1) + 64 * (wv & 2) + wq_row(4 * b)) * (H * DH * 2) + h * (DH * 2);
  asm volatile("" : "+v"(voWq), "+s"(soWq));
  asm volatile("" ::"s"(kc), "s"(vc), "s"(ws), "s"(x), "s"(wq), "s"(wo), "s"(w1), "s"(b1), "s"(w2), "s"(b2), "s"(out));
  const rsrc_t rWq = mk(wq, (ZMASK & 1) || wv < 4 ? 0 : D * H * DH * 2);
  v4u wa0 = __builtin_amdgcn_raw_buffer_load_b128(rWq, voWq, soWq, AUX_F);
  SCHED();
  const unsigned t0 = (unsigned)__builtin_amdgcn_s_memrealtime();
  STAMP(0);
  const rsrc_t rWs = mk(ws, WS_END);

  if (wv >= 4) {
    const int w = wv - 4, lane = threadIdx.x & 63, g = lane >> 4, c = lane & 15;
    if (PRIO_W) __builtin_amdgcn_s_setprio(PRIO_W);
    const rsrc_t rX = mk(x + b * D, D * 2), rWo = mk(wo + (size_t)h * DH * D * 2, (ZMASK & 2) ? 0 : DH * D * 2);
    const int voWo = (16 * g) * (D * 2) + w * 256 + c * 16, soWo = wo_row(4 * b) * (D * 2);
    v4u wa[32], wb[32];
    Tile tb[8];
    wa[0] = wa0;
    auto lda = [&](auto I_) { constexpr int i = I_; wa[i] = __builtin_amdgcn_raw_buffer_load_b128(rWq, voWq, soWq ^ (wq_row(i) * (H * DH * 2)), i < 4 ? AUX_F : AUX_W); };
    auto ldb = [&](auto I_) { constexpr int i = I_; wb[i] = __builtin_amdgcn_raw_buffer_load_b128(rWo, voWo, soWo ^ (wo_row(i) * (D * 2)), i < 4 ? AUX_F : AUX_W); };
    unsigned cpf;
    {
      unsigned long long pc; unsigned len;
      asm volatile("s_getpc_b64 %0\nL_pc_%=:\n\ts_mov_b32 %1, L_fused_decode_end-L_pc_%=" : "=s"(pc), "=s"(len));
      cpf = __builtin_amdgcn_raw_buffer_load_b32(mk((const void*)pc, (int)len), (lane * 128 + (kk * 4 + w) * 384) | (lane < 3 ? 0 : OOB), 0, 0);
    }
    SCHED();
    const unsigned tp = t0 - PFDT / 2 + (unsigned)w * (PFDT / 4);
    wait_tick<PFDT, 1>(tp);
    lda(std::integral_constant<int, 1>{});
    const v4u xrow = __builtin_amdgcn_raw_buffer_load_b128(rX, lane * 16, 0, 0);
    const int cx = c ^ (2 * b), vox = (16 * (cx & 7) + 32 * (cx & 8) + g + 4 * (w & 1) + 64 * (w & 2)) * 2;
    const unsigned xe0 = __builtin_amdgcn_raw_buffer_load_b16(rX, vox, 0, 0), xe1 = __builtin_amdgcn_raw_buffer_load_b16(rX, vox + 16, 0, 0);
    SCHED();
    wait_tick<2 * PFDT, 1>(tp);
    lda(std::integral_constant<int, 2>{});
    unsigned xres = __builtin_amdgcn_raw_buffer_load_b16(rX, (4 * (c & 3) + g) * 2, h * 32, 0);
    unsigned b2v = __builtin_amdgcn_raw_buffer_load_b16(mk(b2, D * 2), (4 * (c & 3) + g) * 2, h * 32, 0);
    SCHED();
    wait_tick<3 * PFDT, 1>(tp);
    lda(std::integral_constant<int, 3>{});
    SCHED();
    const bool member = w == 0, leader = w == 1 && h == 4 * b;
    const int nl = member ? 1 : (leader ? 32 : 0), who = (member ? me : b * 32) + lane, off = lane < nl ? 0 : OOB;
    int voDn = (WS_DN + who * 4) | off, voUp = (WS_UP + who * 128) | off;
    int voPub = (WS_B1 + ((b * 32 + 8 * w + (c >> 1)) * 32 + h) * 64 + (c & 1) * 32 + (g >> 1) * 16) | (g & 1) << 30;
    int voIn = WS_B1 + me * 2048 + lane * 16;
    asm volatile("" : "+v"(voDn), "+v"(voUp), "+v"(voPub), "+v"(voIn));
    v4u tag, t0v, t1v;
    unsigned dn, up;
    auto snapshots = [&]() {
      tag = __builtin_amdgcn_raw_buffer_load_b128(rWs, voPub | ((NOSNAP & 1) ? OOB : 0), 0, AUX_S);
      t0v = __builtin_amdgcn_raw_buffer_load_b128(rWs, voIn | (member && !(NOSNAP & 2) ? 0 : OOB), 0, AUX_S);
      t1v = __builtin_amdgcn_raw_buffer_load_b128(rWs, (voIn | (member && !(NOSNAP & 2) ? 0 : OOB)) + 1024, 0, AUX_S);
      dn = __builtin_amdgcn_raw_buffer_load_b32(rWs, voDn | (leader ? 0 : OOB), 0, AUX_SC1);
      up = __builtin_amdgcn_raw_buffer_load_b32(rWs, voUp | (leader ? 0 : OOB), 0, AUX_SC1);
      SCHED();
    };
    if (SNAPAT == 0) { wait_tick<4 * PFDT, 1>(tp); snapshots(); }
    static_for<0, 4>([&](auto I_) { wait_tick<(4 + I_) * PFDT + (SNAPAT == 0 ? SNAPDT : 0), 1>(tp); ldb(I_); SCHED(); });
    if (SNAPAT == 1) { wait_tick<8 * PFDT, 1>(tp); snapshots(); }
    __builtin_amdgcn_s_barrier();
    unsigned mine = (~up & 1u) | (~dn & 2u);
    if (leader) {
      __builtin_amdgcn_raw_buffer_store_b32(mine | (dn & 4u), rWs, voDn, 0, AUX_SC1);
    }
    __builtin_amdgcn_raw_buffer_store_b128(v4u{SENT, SENT, SENT, SENT}, rWs, (WS_B2 + me * (NCOPY * 64) + lane * 16) | (member && lane < 4 * NCOPY ? 0 : OOB), 0, AUX_SC1);
    *(LAS v4u*)&L.tag3[w][lane] = tag & 1u;
    tag = ~tag & 1u; t0v = ~t0v & 1u; t1v = ~t1v & 1u; xres <<= 16; b2v <<= 16;
    asm volatile("" : "+v"(tag), "+v"(t0v), "+v"(t1v), "+v"(xres), "+v"(b2v) : "v"(cpf) : "memory");
    unsigned xd0, xd1;
    {
      float s = 0.f;
#pragma unroll
      for (int j = 0; j < 4; ++j) s += lo16(xrow[j]) + hi16(xrow[j]);
      const float mean = xrow_sum(row_sum(s)) * (1.f / D);
      float q = 0.f;
#pragma unroll
      for (int j = 0; j < 4; ++j) { const float a = lo16(xrow[j]) - mean, bb = hi16(xrow[j]) - mean; q = fmaf(a, a, fmaf(bb, bb, q)); }
      const float var = rbf(xrow_sum(row_sum(q)) * (1.f / D));
      const float rs = __builtin_amdgcn_rcpf(rbf(__builtin_amdgcn_sqrtf(rbf(var + 1e-5f)))), mb = rbf(mean);
      const float x0 = rbf(lo16(xe0) - mb) * rs, x1 = rbf(lo16(xe1) - mb) * rs;
      xd0 = pk2(x0, x0); xd1 = pk2(x1, x1);
      asm volatile("" : "+v"(xd0), "+v"(xd1));
    }
    {
      v4u msk;
#pragma unroll
      for (int t = 0; t < 4; ++t) msk[t] = (c >> 1) == t ? ((c & 1) ? 0xFFFF0000u : 0x0000FFFFu) : 0u;
      f32x4 qa = {0.f, 0.f, 0.f, 0.f};
      const bool dear = ((((unsigned)(size_t)wq >> 8) + (unsigned)h) & 3u) == 1u;
      const unsigned xq = dear ? XQ : XQL, xo = dear ? XO : XOL;
      unsigned tx = t0 + X0 + (unsigned)w * XSTAG;
      static_assert(XWIN >= 4, "");
      static_for<0, 56>([&](auto I_) {
        constexpr int i = I_;
        if constexpr (i >= 28 + XWIN) asm volatile("" ::"v"(wb[i - XWIN - 24]));
        wait_tick<0, 1>(tx);
        tx += i < 28 ? xq : xo;
        asm volatile("" : "+s"(tx));
        if constexpr (i == 6) { if (member) dn = __builtin_amdgcn_raw_buffer_load_b32(rWs, voDn, 0, AUX_SC1); }
        if constexpr (i == 10) {
          if (member) {
            mine = ~dn & 4u;
            up = (dn & 3u) | mine;
            __builtin_amdgcn_raw_buffer_store_b32(up, rWs, voUp, 0, AUX_SC1);
          }
        }
        if constexpr (i == 31) { if (leader) up = __builtin_amdgcn_raw_buffer_load_b32(rWs, voUp, 0, AUX_SC1); }
        if constexpr (i == 35) {
          if (leader) {
            for (unsigned spins = 0; any(lane < 32 && (up & 3u) != mine) && spins < SPIN_CAP; ++spins) {
              asm volatile("" ::: "memory");
              up = __builtin_amdgcn_raw_buffer_load_b32(rWs, voUp, 0, AUX_SC1);
            }
            __builtin_amdgcn_raw_buffer_store_b32(mine | (up & 4u), rWs, voDn, 0, AUX_SC1);
          }
        }
        if constexpr (i == 43) { if (member) dn = __builtin_amdgcn_raw_buffer_load_b32(rWs, voDn, 0, AUX_SC1); }
        if constexpr (i == 47) {
          if (member) {
            for (unsigned spins = 0; any(lane == 0 && (dn & 4u) != mine) && spins < SPIN_CAP; ++spins) {
              const unsigned nup = (dn & 3u) | mine;
              __builtin_amdgcn_raw_buffer_store_b32(nup, rWs, nup != up ? voUp : OOB, 0, AUX_SC1);
              up = nup;
              DRAIN();
              dn = __builtin_amdgcn_raw_buffer_load_b32(rWs, voDn, 0, AUX_SC1);
            }
          }
        }
        if constexpr (i < 28) lda(std::integral_constant<int, i + 4>{}); else ldb(std::integral_constant<int, i - 24>{});
        SCHED();
        if constexpr (i < 29 && i + 4 - XWIN >= 0) { constexpr int j = i + 4 - XWIN; qa = mv4<(j >> 1)>(j & 1 ? xd1 : xd0, msk, wa[j], qa); asm volatile("" : "+v"(qa)); }
        if constexpr (i == 29) {
          static_for<33 - XWIN, 32>([&](auto J_) { qa = mv4<(J_ >> 1)>(J_ & 1 ? xd1 : xd0, msk, wa[J_], qa); });
          if (g < 2) *(LAS f32x4*)&L.qp[w][8 * c + 4 * g] = qa;
          lds_signal(cnt + 0, lane);
          if (w == 3) {
            lds_wait(cnt + 0, 4u);
            const f32x2 q2 = (*(LAS f32x2*)&L.qp[0][2 * lane] + *(LAS f32x2*)&L.qp[1][2 * lane]) + (*(LAS f32x2*)&L.qp[2][2 * lane] + *(LAS f32x2*)&L.qp[3][2 * lane]);
            L.qb[lane] = pk2(q2.x, q2.y);
            lds_signal(cnt + 1, lane);
          }
        }
        if constexpr (i == 1) tb[0] = mk_tile(wb[0], wb[1], wb[2], wb[3]);
        if constexpr (i >= 28 + XWIN && (i - XWIN) % 4 == 3) { constexpr int k = (i - XWIN - 24) / 4; tb[k] = mk_tile(wb[4 * k], wb[4 * k + 1], wb[4 * k + 2], wb[4 * k + 3]); }
        SCHED();
      });
      NEW_CODE_PAGE();
      static_for<(32 - XWIN) / 4, 8>([&](auto K_) { constexpr int k = K_; tb[k] = mk_tile(wb[4 * k], wb[4 * k + 1], wb[4 * k + 2], wb[4 * k + 3]); });
    }
    int aO = (c >> 3) * 576 + g * 16, sidx = (c & 3) * 0x5555;
    int voH = (c >> 2) < NCOPY ? WS_B2 + (b * NCOPY + (c >> 2)) * 2048 + h * 64 + (4 * g + (c & 3)) * 4 : OOB, voO = c < 4 ? h * 32 + (4 * c + g) * 2 : OOB;
    const rsrc_t rO = mk(out + b * D, D * 2);
    f32x4 ylo = {0.f, 0.f, 0.f, 0.f}, yhi = {0.f, 0.f, 0.f, 0.f};
    asm volatile("" : "+v"(voIn), "+v"(voH), "+v"(voO), "+v"(aO), "+v"(ylo), "+v"(yhi), "+v"(sidx), "+v"(voPub) : "s"(rO));
    __builtin_amdgcn_s_setprio(2);
    SCHED();

    lds_barrier();
    STAMP(5);
    {
      v4u ao[8];
#pragma unroll
      for (int s = 0; s < 8; ++s) ao[s] = *(LAS v4u*)((LAS char*)&L.ot[0][0] + aO + 64 * s);
      SCHED();
#pragma unroll
      for (int s = 0; s < 8; ++s) { ylo = smv(ao[s], tb[s].lo, sidx, ylo); yhi = smv(ao[s], tb[s].hi, sidx, yhi); }
      v4u y;
#pragma unroll
      for (int t = 0; t < 4; ++t) y[t] = __float_as_uint(swap32_add(ylo[t], yhi[t]));
      __builtin_amdgcn_raw_buffer_store_b128((y & ~1u) | tag, rWs, voPub, 0, AUX_SC1);
    }
    SCHED();
    STAMP(6);
    if (w != 0) asm volatile("s_endpgm" ::: "memory");

    unsigned npoll = 0;
    auto gather = [&]() -> float {
      v4u a0, a1;
      unsigned spins = 0;
      auto look = [&]() {
        a0 = __builtin_amdgcn_raw_buffer_load_b128(rWs, voIn, 0, AUX_SC1) ^ t0v;
        a1 = __builtin_amdgcn_raw_buffer_load_b128(rWs, voIn + 1024, 0, AUX_SC1) ^ t1v;
        return any(((a0.x | a0.y | a0.z | a0.w | a1.x | a1.y | a1.z | a1.w) & 1u) != 0u);
      };
      while (look()) {
        asm volatile("" : "+s"(spins)::"memory");
        if (++spins > SPIN_CAP) break;
      }
      npoll = spins + 1;
      const f32x4 s = __builtin_bit_cast(f32x4, a0) + __builtin_bit_cast(f32x4, a1);
      float v = swap16_add(swap32_add(s.x, s.z), swap32_add(s.y, s.w));
      v += dppf<ROR4>(v); v += dppf<ROR8>(v);
      return v;
    };
    if (DLY1) __builtin_amdgcn_s_sleep(DLY1);
    asm volatile("" ::: "memory");
    STAMP(1);
    const unsigned hb = hi_bf(__uint_as_float(xres) + __uint_as_float(hi_bf(gather())));
    __builtin_amdgcn_raw_buffer_store_b32(hb, rWs, voH, 0, AUX_SC1);
    if (NCOPY == 8) __builtin_amdgcn_raw_buffer_store_b32(hb, rWs, voH, 8192, AUX_SC1);
    SCHED();
    STAMP(7);
    if (DLY2) __builtin_amdgcn_s_sleep(DLY2);
    __builtin_amdgcn_s_barrier();
    SCHED();
    float hres = __uint_as_float(hb) + __uint_as_float(b2v);
    t0v ^= 1u; t1v ^= 1u;
    asm volatile("" : "+v"(hres), "+v"(t0v), "+v"(t1v));
    __builtin_amdgcn_s_barrier();
    if (DLY3) __builtin_amdgcn_s_sleep(DLY3);
    asm volatile("" ::: "memory");
    const float ov = hres + gather();
    __builtin_amdgcn_raw_buffer_store_b16((unsigned short)pk2(ov, ov), rO, voO, 0, AUX_O);
    STAMP(11);
    asm volatile("s_endpgm" ::: "memory");
  }

  {
    int tid = threadIdx.x;
    asm volatile("" : "+v"(tid));
    const int lane = tid & 63, g = lane >> 4, c = lane & 15;
    if (tid < 4) cnt[tid] = 0u;
    const size_t kvbase = ((size_t)me * SK + (SK - WIN_KEYS)) * (DH * 2);
    const rsrc_t rK = mk(kc + kvbase, WIN_KEYS * DH * 2), rV = mk(vc + kvbase, WIN_KEYS * DH * 2);
    int so = wv << 14 | (me & 1) << 10 | (me & 2) << 11 | (me & 4) << 9 | (me & 8) << 10;
    const int vo16 = lane * 16;
    v4u kv[128];
    auto ldkv = [&](auto S_) {
      constexpr int s = S_;
      kv[s] = __builtin_amdgcn_raw_buffer_load_b128(s < 64 ? rK : rV, vo16, so, AUX_NT);
      so ^= piece_off(s) ^ piece_off(s + 1);
      asm volatile("" : "+s"(so));
    };
    const int u0 = (43 * h) >> 3, nu = ((43 * (h + 1)) >> 3) - u0;
    const rsrc_t rW1 = mk(w1, (ZMASK & 4) ? 0 : D * F * 2), rW2 = mk(w2 + (size_t)u0 * 8 * D * 2, (ZMASK & 8) ? 0 : nu * 8 * D * 2);
    const int rq = b & 1, r1 = b >> 1, r2 = r1 == 3 ? 0 : r1;
    const int s2 = (c >> 2) + r2, cq = c < 12 ? 4 * (s2 >= 3 ? s2 - 3 : s2) + ((c & 3) ^ rq) : 14;
    unsigned b1v = __builtin_amdgcn_raw_buffer_load_b16(mk(b1 + u0 * 8, nu * 16), (4 * cq + g) * 2, 0, 0);
    v4u msk;
#pragma unroll
    for (int t = 0; t < 4; ++t) msk[t] = (c >> 1) == t ? ((c & 1) ? 0xFFFF0000u : 0x0000FFFFu) : 0u;
    asm volatile("" : "+v"(msk));
    const unsigned ph = ((unsigned)(bid * 4 + wv) * 79u) & 255u;
    const unsigned tkv = t0 + K0 - ((ph * PACE) >> 15);
    int lim = 0;
    lds_barrier();
    static_for<0, WIN>([&](auto S_) { wait_gap<kv_tick(S_), GAPK, 0>(tkv, lim); ldkv(S_); SCHED(); });
    f32x4 oacc = {0.f, 0.f, 0.f, 0.f};
    v4u qpk, pk[4];
    float ys[16], sc[4], mloc, lsum;
    unsigned pp[4];
    constexpr float LOG2E = 1.4426950408889634f;
    constexpr int GPARK = NPARK / 4, GREG = 16 - GPARK, KDONE = QSLOT + 16, VSLOT = KDONE + 10;
    static_assert(QSLOT + GREG >= 63 && NPARK % 4 == 0, "");
    LAS v4u* park = (LAS v4u*)&L.park[wv][0][lane];
    v4u wu[16], wd[12];
    int voW1[2], voW2, aOt[4], soU[8], soD[6];
    auto ldu = [&](auto I_) { constexpr int i = I_; wu[i] = __builtin_amdgcn_raw_buffer_load_b128(rW1, voW1[i & 1], soU[i >> 1], AUX_M); };
    auto ldd = [&](auto I_) { constexpr int i = I_; wd[i] = __builtin_amdgcn_raw_buffer_load_b128(rW2, voW2 + (i & 2) * (D * 2), soD[2 * (i >> 2) + (i & 1)], AUX_M); };
    v4u ah[4], tag;
    int sidx, voPub, voHp, aU, aG, aR, sh[4];
    f32x4 gram, ylo, yhi, zlo, zhi;
    static_for<0, 128>([&](auto S_) {
      constexpr int s = S_;
      if constexpr (s + WIN < 128) {
        asm volatile("" ::"v"(kv[s]));
        wait_gap<kv_tick(s + WIN), GAPK, ((s + 1) * NAPK >> 2) - (s * NAPK >> 2)>(tkv, lim);
        ldkv(std::integral_constant<int, s + WIN>{});
      }
      if constexpr (s == 124) {
        wait_tick<K0 + kv_tick(127) + SHD, 1>(t0);
        static_for<0, 2>(ldu);
        ldd(std::integral_constant<int, 0>{});
        ldd(std::integral_constant<int, 2>{});
      }
      SCHED();
      if constexpr (s == ALIGNK) NEW_CODE_PAGE();
      if constexpr (s < NPARK) park[s * 64] = kv[s];
      if constexpr (s == QSLOT) {
        lds_wait(cnt + 1, 1u);
        qpk = *(LAS v4u*)&L.qb[4 * c];
        STAMP(12);
      }
      if constexpr (s > QSLOT && s <= KDONE) {
        constexpr int n = s - QSLOT - 1, gr = n < GREG ? GPARK + n : n - GREG, u = 4 * gr;
        if constexpr (gr < GPARK) ys[gr] = score4(pk[0], pk[1], pk[2], pk[3], qpk);
        else ys[gr] = score4(kv[u], kv[u + 1], kv[u + 2], kv[u + 3], qpk);
        asm volatile("" : "+v"(ys[gr]));
        SCHED();
        if constexpr (n + 1 >= GREG && n + 1 < 16) {
#pragma unroll
          for (int k = 0; k < 4; ++k) pk[k] = park[(4 * (n + 1 - GREG) + k) * 64];
        }
      }
      if constexpr (s == KDONE + 1 || s == KDONE + 2) {
#pragma unroll
        for (int i = 2 * (s - KDONE - 1); i < 2 * (s - KDONE); ++i) {
          float t[4];
#pragma unroll
          for (int p = 0; p < 4; ++p) t[p] = ys[4 * i + p] + dppf<XOR1>(ys[4 * i + p]);
          const float e = (c & 1) ? t[1] : t[0], f = (c & 1) ? t[3] : t[2];
          const float e2 = e + dppf<XOR2>(e), f2 = f + dppf<XOR2>(f);
          sc[i] = rbf(rbf((c & 2) ? f2 : e2) * 0.08838834764831845f) * LOG2E;
          asm volatile("" : "+v"(sc[i]));
        }
      }
      if constexpr (s == KDONE + 3) { mloc = row_max(fmaxf(fmaxf(sc[0], sc[1]), fmaxf(sc[2], sc[3]))); asm volatile("" : "+v"(mloc)); }
      if constexpr (s == KDONE + 4) {
        mloc = xrow_max(mloc);
#pragma unroll
        for (int i = 0; i < 4; ++i) { sc[i] = __builtin_amdgcn_exp2f(sc[i] - mloc); asm volatile("" : "+v"(sc[i])); }
      }
      if constexpr (s == KDONE + 5) { lsum = xrow_sum(row_sum((sc[0] + sc[1]) + (sc[2] + sc[3]))); asm volatile("" : "+v"(lsum)); }
      if constexpr (s == KDONE + 6) {
        if (lane == 0) *(LAS f32x2*)&L.ml[wv][0] = f32x2{mloc, lsum};
        lds_signal(cnt + 2, lane);
      }
      if constexpr (s == KDONE + 8) lds_wait(cnt + 2, 4u);
      if constexpr (s == KDONE + 9) {
        const f32x2 e = *(LAS f32x2*)&L.ml[lane & 3][0];
        float M = e.x;
        M = fmaxf(M, dppf<XOR1>(M)); M = fmaxf(M, dppf<XOR2>(M));
        lsum = e.y * __builtin_amdgcn_exp2f(e.x - M);
        mloc = __builtin_amdgcn_exp2f(mloc - M);
        asm volatile("" : "+v"(lsum), "+v"(mloc));
      }
      if constexpr (s == VSLOT) {
        lsum += dppf<XOR1>(lsum); lsum += dppf<XOR2>(lsum);
        const float f = mloc * __builtin_amdgcn_rcpf(lsum);
#pragma unroll
        for (int i = 0; i < 4; ++i) { pp[i] = pk2(sc[i] * f, sc[i] * f); asm volatile("" : "+v"(pp[i])); }
      }
      if constexpr (s > VSLOT) {
        constexpr int lo = 64 + CATCH * (s - 1 - VSLOT) < s ? 64 + CATCH * (s - 1 - VSLOT) : s, hi = 64 + CATCH * (s - VSLOT) < s + 1 ? 64 + CATCH * (s - VSLOT) : s + 1;
        static_for<lo, hi>([&](auto U_) { constexpr int u = U_; oacc = mv4<4 * (u & 3) + ((u >> 2) & 3)>(pp[(u - 64) >> 4], msk, kv[u], oacc); });
      }
      if constexpr (s == 113) {
        voW1[0] = (c & 7) < nu ? (128 * wv + 16 * (c >> 3) + 8 * (g & 1) + (g >> 1)) * (F * 2) + (u0 + (c & 7)) * 16 : OOB;
        voW1[1] = voW1[0] + 4 * (F * 2);
        voW2 = (8 * (g & 1) + (g >> 1)) * (D * 2) + wv * 256 + c * 16;
        asm volatile("" : "+v"(voW1[0]), "+v"(voW1[1]), "+v"(voW2));
      }
      if constexpr (s == 114) {
#pragma unroll
        for (int k = 0; k < 8; ++k) { soU[k] = (32 * ((k >> 1) ^ r1) + 2 * ((k & 1) ^ rq)) * (F * 2); asm volatile("" : "+s"(soU[k])); }
      }
      if constexpr (s == 115) {
#pragma unroll
        for (int k = 0; k < 6; ++k) { const int t = (k >> 1) + r2; soD[k] = (16 * (t >= 3 ? t - 3 : t) + 4 * ((k & 1) ^ rq)) * (D * 2); asm volatile("" : "+s"(soD[k])); }
#pragma unroll
        for (int k = 0; k < 4; ++k) { sh[k] = (k ^ r1) * 128; asm volatile("" : "+s"(sh[k])); }
      }
      if constexpr (s == 116) {
#pragma unroll
        for (int t = 0; t < 4; ++t) { aOt[t] = (g < 2 ? (wv >> 1) * 576 : 2 * 576) + ((32 * t + (lane & 31)) ^ (16 * b)) * 4 + (wv & 1) * 2; asm volatile("" : "+v"(aOt[t])); }
        sidx = (c & 3) * 0x5555;
        voHp = WS_B2 + (b * NCOPY + ((h >> 2) & (NCOPY - 1))) * 2048 + wv * 512 + (c >> 3) * 64 + (g ^ (2 * rq)) * 16;
        asm volatile("" : "+v"(sidx), "+v"(voHp));
      }
      if constexpr (s == 117) {
        voPub = (g & 1) == 0 ? WS_B1 + ((b * 32 + 8 * wv + (c >> 1)) * 32 + h) * 64 + (c & 1) * 32 + (g >> 1) * 16 : OOB;
        aR = (g >> 1) * 512 + (g & 1) * 256 + ((cq >> 1) + 8 * (cq & 1)) * 16;
        tag = *(LAS v4u*)&L.tag3[wv][lane];
        b1v <<= 16;
        asm volatile("" : "+v"(voPub), "+v"(aR), "+v"(tag), "+v"(b1v));
      }
      if constexpr (s == 118) {
        const bool match = (g >> 1) == (c >> 3);
        const int rec = (2 * wv + (g >> 1)) * 512 + (g & 1) * 256;
        aU = (match && (c & 7) != 7 ? rec : 8 * 512) + (c & 7) * 16;
        aG = match && (c & 7) == 0 ? rec + 7 * 16 : 8 * 512 + (c & 7) * 16;
        asm volatile("" : "+v"(aU), "+v"(aG));
      }
      if constexpr (s == 119) {
#pragma unroll
        for (int k = 0; k < 4; ++k) { ah[k] = v4u{0x3F800000u, 0x3F800000u, 0x3F800000u, 0x3F800000u}; asm volatile("" : "+v"(ah[k])); }
        gram = f32x4{0.f, 0.f, 0.f, 0.f}; ylo = gram; yhi = gram; zlo = gram; zhi = gram;
        asm volatile("" : "+v"(gram), "+v"(ylo), "+v"(yhi), "+v"(zlo), "+v"(zhi));
      }
      SCHED();
    });
    {
      const unsigned p01 = pk2(oacc.x, oacc.y), p23 = pk2(oacc.z, oacc.w);
      LAS char* po = (LAS char*)&L.ot[0][0];
      *(LAS unsigned short*)(po + aOt[0]) = (unsigned short)p01; *(LAS unsigned short*)(po + aOt[1]) = (unsigned short)(p01 >> 16);
      *(LAS unsigned short*)(po + aOt[2]) = (unsigned short)p23; *(LAS unsigned short*)(po + aOt[3]) = (unsigned short)(p23 >> 16);
    }
    STAMP(14);
    lds_barrier();
    SCHED();
    if (DLYU) __builtin_amdgcn_s_sleep(DLYU);
    static_for<2, 16>([&](auto I_) { ldu(I_); if constexpr (MWIN != 0) asm volatile("s_waitcnt vmcnt(%0)" ::"n"(MWIN) : "memory"); });
    ldd(std::integral_constant<int, 1>{});
    ldd(std::integral_constant<int, 3>{});
    SCHED();
    Tile tu[4], td[3];
#pragma unroll
    for (int k = 0; k < 4; ++k) tu[k] = mk_tile(wu[4 * k], wu[4 * k + 1], wu[4 * k + 2], wu[4 * k + 3]);
    td[0] = mk_tile(wd[0], wd[1], wd[2], wd[3]);
    unsigned long long em = 0x0F0F0F0F0F0F0F0Full;
    float kinv = 1.f / D, keps = 1e-5f, kg1 = -2.f * 0.7978845608028654f * 1.4426950408889634f;
    asm volatile("" : "+s"(em), "+v"(kinv), "+v"(keps), "+v"(kg1));
    STAMP(13);
    SCHED();
    __builtin_amdgcn_s_barrier();
    {
      asm volatile("buffer_load_dwordx4 %0, %8, %9, %10 offen" MPOL "\n\tbuffer_load_dwordx4 %1, %8, %9, %11 offen" MPOL "\n\t"
                   "buffer_load_dwordx4 %2, %8, %9, %10 offen offset:2048" MPOL "\n\tbuffer_load_dwordx4 %3, %8, %9, %11 offen offset:2048" MPOL "\n\t"
                   "buffer_load_dwordx4 %4, %8, %9, %12 offen" MPOL "\n\tbuffer_load_dwordx4 %5, %8, %9, %13 offen" MPOL "\n\t"
                   "buffer_load_dwordx4 %6, %8, %9, %12 offen offset:2048" MPOL "\n\tbuffer_load_dwordx4 %7, %8, %9, %13 offen offset:2048" MPOL
                   : "=&v"(wd[4]), "=&v"(wd[5]), "=&v"(wd[6]), "=&v"(wd[7]), "=&v"(wd[8]), "=&v"(wd[9]), "=&v"(wd[10]), "=&v"(wd[11])
                   : "v"(voW2), "s"(rW2), "s"(soD[2]), "s"(soD[3]), "s"(soD[4]), "s"(soD[5]) : "memory");
      asm volatile("s_sleep %[nap]\n\t"
                   "s_mov_b64 exec, %[em]\n\t"
                   "buffer_load_dwordx4 %[a0], %[vo], %[rs], %[s0] offen sc1\n\t"
                   "buffer_load_dwordx4 %[a1], %[vo], %[rs], %[s1] offen sc1\n\t"
                   "buffer_load_dwordx4 %[a2], %[vo], %[rs], %[s2] offen sc1\n\t"
                   "buffer_load_dwordx4 %[a3], %[vo], %[rs], %[s3] offen sc1\n\t"
                   "s_mov_b64 exec, -1\n\t"
                   "s_waitcnt vmcnt(8)"
                   : [a0] "+v"(ah[0]), [a1] "+v"(ah[1]), [a2] "+v"(ah[2]), [a3] "+v"(ah[3])
                   : [vo] "v"(voHp), [rs] "s"(rWs), [s0] "s"(sh[0]), [s1] "s"(sh[1]), [s2] "s"(sh[2]), [s3] "s"(sh[3]), [em] "s"(em), [nap] "n"(DLY2B) : "memory");
      asm volatile("" : "+v"(wd[4]), "+v"(wd[5]), "+v"(wd[6]), "+v"(wd[7]));
      td[1] = mk_tile(wd[4], wd[5], wd[6], wd[7]);
      asm volatile("s_waitcnt vmcnt(4)" : "+v"(wd[8]), "+v"(wd[9]), "+v"(wd[10]), "+v"(wd[11])::"memory");
      td[2] = mk_tile(wd[8], wd[9], wd[10], wd[11]);
      asm volatile("s_waitcnt vmcnt(0)" : "+v"(ah[0]), "+v"(ah[1]), "+v"(ah[2]), "+v"(ah[3])::"memory");
      unsigned spins = 0;
      auto missing = [&]() { return any((((ah[0].x | ah[0].y | ah[0].z) | (ah[0].w | ah[1].x) | (ah[1].y | ah[1].z) | (ah[1].w | ah[2].x) | (ah[2].y | ah[2].z) | (ah[2].w | ah[3].x) | (ah[3].y | ah[3].z) | ah[3].w) & 1u) != 0u); };
      if (__builtin_expect(missing(), 0)) {
        do {
          asm volatile("s_mov_b64 exec, %[em]\n\t"
                   "buffer_load_dwordx4 %[a0], %[vo], %[rs], %[s0] offen sc1\n\t"
                   "buffer_load_dwordx4 %[a1], %[vo], %[rs], %[s1] offen sc1\n\t"
                   "buffer_load_dwordx4 %[a2], %[vo], %[rs], %[s2] offen sc1\n\t"
                   "buffer_load_dwordx4 %[a3], %[vo], %[rs], %[s3] offen sc1\n\t"
                   "s_mov_b64 exec, -1\n\t"
                   "s_waitcnt vmcnt(0)"
                   : [a0] "+v"(ah[0]), [a1] "+v"(ah[1]), [a2] "+v"(ah[2]), [a3] "+v"(ah[3])
                   : [vo] "v"(voHp), [rs] "s"(rWs), [s0] "s"(sh[0]), [s1] "s"(sh[1]), [s2] "s"(sh[2]), [s3] "s"(sh[3]), [em] "s"(em) : "memory");
        } while (missing() && ++spins < SPIN_CAP);
        ++spins;
      }
      STAMP(8);
#pragma unroll
      for (int s = 0; s < 4; ++s) gram = __builtin_amdgcn_mfma_f32_16x16x32_bf16(__builtin_bit_cast(bf16x8, ah[s]), __builtin_bit_cast(bf16x8, ah[s]), gram, 0, 0, 0);
      SCHED();
#pragma unroll
      for (int s = 0; s < 4; ++s) { ylo = smv(ah[s], tu[s].lo, sidx, ylo); SCHED(); }
#pragma unroll
      for (int s = 0; s < 4; ++s) { yhi = smv(ah[s], tu[s].hi, sidx, yhi); SCHED(); }
      *(LAS f32x4*)((LAS char*)&L.up[0][0] + aG) = gram;
      *(LAS f32x4*)((LAS char*)&L.up[0][0] + aU) = ylo;
      *(LAS f32x4*)((LAS char*)&L.up[0][0] + aU + 128) = yhi;
    }
    lds_barrier();
    STAMP(9);
    {
      const LAS char* pu = (const LAS char*)&L.up[0][0] + aR;
      const f32x4 p = (*(const LAS f32x4*)pu + *(const LAS f32x4*)(pu + 1024)) + (*(const LAS f32x4*)(pu + 2048) + *(const LAS f32x4*)(pu + 3072));
      const auto uc = __builtin_amdgcn_permlane16_swap(__float_as_uint(swap32_add(p.x, p.z)), __float_as_uint(swap32_add(p.y, p.w)), false, false);
      const float ur = __uint_as_float(uc[0]), cs = __uint_as_float(uc[1]);
      const float mean = dppf<BCAST + 15>(cs) * kinv;
      const float rstd = __builtin_amdgcn_rsqf(fmaxf(fmaf(-mean, mean, fmaf(dppf<BCAST + 15>(ur), kinv, keps)), 1e-5f));
      const float u = fmaf(fmaf(-mean, cs, ur), rstd, __uint_as_float(b1v));
      constexpr float G3 = -2.f * 0.7978845608028654f * 1.4426950408889634f * 0.044715f;
      const float e = __builtin_amdgcn_exp2f(u * fmaf(u * u, G3, kg1));
      const unsigned gd = hi_bf(u * __builtin_amdgcn_rcpf(1.f + e));
      v4u a[3];
      a[0] = v4u{dppu<BCAST>(gd), dppu<BCAST + 1>(gd), dppu<BCAST + 2>(gd), dppu<BCAST + 3>(gd)};
      a[1] = v4u{dppu<BCAST + 4>(gd), dppu<BCAST + 5>(gd), dppu<BCAST + 6>(gd), dppu<BCAST + 7>(gd)};
      SCHED();
      zlo = smv(a[0], td[0].lo, sidx, zlo); zhi = smv(a[0], td[0].hi, sidx, zhi);
      SCHED();
      a[2] = v4u{dppu<BCAST + 8>(gd), dppu<BCAST + 9>(gd), dppu<BCAST + 10>(gd), dppu<BCAST + 11>(gd)};
      SCHED();
      zlo = smv(a[1], td[1].lo, sidx, zlo); zhi = smv(a[1], td[1].hi, sidx, zhi);
      zlo = smv(a[2], td[2].lo, sidx, zlo); zhi = smv(a[2], td[2].hi, sidx, zhi);
      v4u y;
#pragma unroll
      for (int t = 0; t < 4; ++t) y[t] = __float_as_uint(lane < 32 ? zlo[t] : zhi[t]);
      __builtin_amdgcn_raw_buffer_store_b128((y & ~1u) | tag, rWs, voPub, 0, AUX_SC1);
    }
    STAMP(10);
    asm volatile("s_endpgm\nL_fused_decode_end:" ::: "memory");
  }
}

}

extern "C" void kernel_launch(void* const* d_in, const int* in_sizes, int n_in, void* d_out, int out_size, void* d_ws, size_t ws_size,
                              hipStream_t stream) {
  fused_decode<<<256, 512, 0, stream>>>((const unsigned char*)d_in[1], (const unsigned char*)d_in[2], (unsigned char*)d_ws,
                                        (const unsigned short*)d_in[0], (const unsigned char*)d_in[3], (const unsigned char*)d_in[4],
                                        (const unsigned char*)d_in[6], (const unsigned short*)d_in[7], (const unsigned char*)d_in[8],
                                        (const unsigned short*)d_in[9], (unsigned short*)d_out);
}
```
